```python
import torch

def _softmax(x, axis=-1):
    return torch.softmax(x, dim=axis)
def _gelu(x):
    return torch.nn.functional.gelu(x, approximate="tanh")
def _silu(x):
    return torch.nn.functional.silu(x)
def _sigmoid(x):
    return torch.sigmoid(x)
def _layernorm(x, axis=-1, eps=1e-5):
    m = x.mean(dim=axis, keepdim=True)
    v = x.var(dim=axis, keepdim=True, unbiased=False)
    return (x - m) / torch.sqrt(v + eps)
def _scatter_add(tgt, idx, src, axis=0):
    return tgt.clone().index_add_(axis, idx, src)
def _segment_sum(src, seg, n):
    out = torch.zeros((n,) + src.shape[1:], dtype=src.dtype)
    return out.index_add_(0, seg, src)
def _pad(x, pw):
    flat = tuple(v for pair in reversed(pw) for v in pair)
    return torch.nn.functional.pad(x, flat)

kernel_name = 'standalone__dense_activation_squared_relu__prompt'

def setup_inputs():

    _w = (lambda a: (lambda t: t.to(torch.bfloat16) if t.is_floating_point() else t)(torch.as_tensor(a)))
    B, S, D, F = 1, 2048, 640, 1728
    return {
        "x": _w((lambda *s: torch.randn(*s, dtype=torch.bfloat16) * 0.05)(B, S, D)),
        "w1": _w((lambda *s: torch.randn(*s, dtype=torch.bfloat16) * 0.05)(D, F)),
        "b1": _w((lambda *s: torch.randn(*s, dtype=torch.bfloat16) * 0.05)(F)),
        "w2": _w((lambda *s: torch.randn(*s, dtype=torch.bfloat16) * 0.05)(F, D)),
        "b2": _w((lambda *s: torch.randn(*s, dtype=torch.bfloat16) * 0.05)(D)),
    }

def reference(x, w1, b1, w2, b2):
    h = torch.einsum("bsd,df->bsf", x, w1) + b1
    zero = torch.zeros_like(h)
    r = torch.where(h > zero, h, zero)
    h = r * r
    out = torch.einsum("bsf,fd->bsd", h, w2) + b2
    return out
```

```cpp
#include <hip/hip_runtime.h>
#include <hip/hip_bf16.h>
#include <stdint.h>

using bf16 = __hip_bfloat16;
using bf16x8 = __attribute__((ext_vector_type(8))) short;
using s16x4 = __attribute__((ext_vector_type(4))) short;
using f32x4 = __attribute__((ext_vector_type(4))) float;
using u32x4 = __attribute__((ext_vector_type(4))) unsigned;
using u32x2 = __attribute__((ext_vector_type(2))) unsigned;
typedef short v4i16_t __attribute__((ext_vector_type(4)));
typedef __attribute__((address_space(3))) const char* lds_cptr;

constexpr int M_ = 2048, D_ = 640, F_ = 1728;

#define WAIT_V(n) asm volatile("s_waitcnt vmcnt(" #n ")" ::: "memory")
#define BAR() __builtin_amdgcn_s_barrier()

__device__ __forceinline__ s16x4 tr_read(const char* p) {
  return __builtin_bit_cast(s16x4, __builtin_amdgcn_ds_read_tr16_b64_v4i16(
                                       (__attribute__((address_space(3))) v4i16_t*)(lds_cptr)p));
}
__device__ __forceinline__ bf16x8 cat8(s16x4 lo, s16x4 hi) {
  return (bf16x8){lo[0], lo[1], lo[2], lo[3], hi[0], hi[1], hi[2], hi[3]};
}
__device__ __forceinline__ unsigned cvt_pk(float lo, float hi) {
  unsigned r;
  asm("v_cvt_pk_bf16_f32 %0, %1, %2" : "=v"(r) : "v"(lo), "v"(hi));
  return r;
}
__device__ __forceinline__ void glds16(const void* sbase, unsigned voff, unsigned lds_addr) {
  unsigned sv;
  asm volatile("s_mov_b32 %0, m0\n\ts_mov_b32 m0, %3\n\ts_nop 0\n\tglobal_load_lds_dwordx4 %1, %2\n\ts_mov_b32 m0, %0"
               : "=&s"(sv)
               : "v"(voff), "s"(sbase), "s"(lds_addr)
               : "memory");
}
__device__ __forceinline__ float bf2f(unsigned short v) { return __uint_as_float(((unsigned)v) << 16); }

constexpr int G1_NST = 4;
constexpr int G1_STAGE = 32768;

__global__ __launch_bounds__(256, 1) void gemm1_kernel(const bf16* __restrict__ x, const bf16* __restrict__ w1,
                                                       const bf16* __restrict__ b1, bf16* __restrict__ H) {
  __shared__ __attribute__((aligned(16))) char smem[G1_NST * G1_STAGE];
  const int tid = threadIdx.x, lane = tid & 63;
  const int w = __builtin_amdgcn_readfirstlane(tid >> 6);
  const int wm = w >> 1, wn = w & 1;
  const int bid = blockIdx.x;
  const int xcd = bid & 7, idx = bid >> 3;
  const int mt = (xcd >> 1) * 4 + (idx & 3);
  const int nt = (xcd & 1) * 7 + (idx >> 2);
  const int m0 = mt * 128, n0 = nt * 128;

  const unsigned lds0 = (unsigned)(uintptr_t)smem;
  unsigned aoff[4], woff[4];
#pragma unroll
  for (int i = 0; i < 4; ++i) {
    const int piece = 4 * w + i;
    const int row = 8 * piece + (lane >> 3);
    const int chunk = (lane & 7) ^ ((row >> 1) & 7);
    aoff[i] = (unsigned)(row * D_ + chunk * 8) * 2u;
    const int kr = 4 * piece + (lane >> 4);
    int ch = (lane & 15) ^ ((kr & 1) | ((kr & 2) << 1) | (kr & 8));
    if (nt == 13) ch &= 7;
    woff[i] = (unsigned)(kr * F_ + ch * 8) * 2u;
  }
  const bf16* xbase = x + (size_t)m0 * D_;
  const bf16* wbase = w1 + n0;
  auto stage = [&](int st) {
    const unsigned sb = lds0 + (st % G1_NST) * G1_STAGE + 4 * w * 1024;
#pragma unroll
    for (int i = 0; i < 4; ++i) glds16(xbase + st * 64, aoff[i], sb + i * 1024);
#pragma unroll
    for (int i = 0; i < 4; ++i) glds16(wbase + (size_t)st * 64 * F_, woff[i], sb + 16384 + i * 1024);
  };

  const int ml = lane & 15, g = lane >> 4, q = (lane >> 2) & 3, p = lane & 3;
  const int aswz = (ml >> 1) & 7;
  const int a_off0 = (wm * 64 + ml) * 128 + ((g ^ aswz) * 16);
  const int a_off1 = (wm * 64 + ml) * 128 + (((4 + g) ^ aswz) * 16);
  const int wmask = (q & 1) | ((q & 2) << 1) | ((g & 1) << 3);
  int w_off[4];
#pragma unroll
  for (int b = 0; b < 4; ++b) {
    const int c = b >> 1;
    const int ch16 = (wn * 8 + 4 * c + p) ^ wmask;
    w_off[b] = 16384 + (8 * g + q) * 256 + ch16 * 16 + 8 * ((b & 1) ^ (p & 1));
  }

  f32x4 acc[4][4];
  {
    const int nb = n0 + wn * 64;
#pragma unroll
    for (int b = 0; b < 4; ++b) {
      const int nl = 8 * (4 * (b >> 1) + g) + 4 * ((b & 1) ^ (g & 1));
      f32x4 bv;
      if (nb + nl < F_) {
        const s16x4 bb = *reinterpret_cast<const s16x4*>(b1 + nb + nl);
        bv = (f32x4){bf2f(bb[0]), bf2f(bb[1]), bf2f(bb[2]), bf2f(bb[3])};
      } else {
        bv = (f32x4){0.f, 0.f, 0.f, 0.f};
      }
#pragma unroll
      for (int mb = 0; mb < 4; ++mb) acc[b][mb] = bv;
    }
  }

  stage(0);
  stage(1);
  stage(2);

  constexpr int NKT = D_ / 64;
#pragma unroll
  for (int kt = 0; kt < NKT; ++kt) {
    if (kt + 2 < NKT) { WAIT_V(16); }
    else if (kt + 1 < NKT) { WAIT_V(8); }
    else { WAIT_V(0); }
    BAR();
    if (kt + 3 < NKT) stage(kt + 3);
    const char* sb = smem + (kt % G1_NST) * G1_STAGE;
#pragma unroll
    for (int s = 0; s < 2; ++s) {
      bf16x8 af[4], wf[4];
#pragma unroll
      for (int mb = 0; mb < 4; ++mb)
        af[mb] = *reinterpret_cast<const bf16x8*>(sb + (s ? a_off1 : a_off0) + mb * 2048);
#pragma unroll
      for (int b = 0; b < 4; ++b)
        wf[b] = cat8(tr_read(sb + w_off[b] + s * 8192), tr_read(sb + w_off[b] + s * 8192 + 1024));
#pragma unroll
      for (int b = 0; b < 4; ++b)
#pragma unroll
        for (int mb = 0; mb < 4; ++mb)
          acc[b][mb] = __builtin_amdgcn_mfma_f32_16x16x32_bf16(wf[b], af[mb], acc[b][mb], 0, 0, 0);
    }
  }

  if (n0 + wn * 64 < F_) {
#pragma unroll
    for (int mb = 0; mb < 4; ++mb) {
      bf16* hrow = H + (size_t)(m0 + wm * 64 + mb * 16 + ml) * F_ + n0 + wn * 64;
#pragma unroll
      for (int c = 0; c < 2; ++c) {
        const f32x4 e = acc[2 * c][mb], o = acc[2 * c + 1][mb];
        f32x4 lo, hi;
#pragma unroll
        for (int r = 0; r < 4; ++r) {
          lo[r] = (g & 1) ? o[r] : e[r];
          hi[r] = (g & 1) ? e[r] : o[r];
        }
#pragma unroll
        for (int r = 0; r < 4; ++r) {
          const float a = fmaxf(lo[r], 0.f), bq = fmaxf(hi[r], 0.f);
          lo[r] = a * a;
          hi[r] = bq * bq;
        }
        u32x4 v = {cvt_pk(lo[0], lo[1]), cvt_pk(lo[2], lo[3]), cvt_pk(hi[0], hi[1]), cvt_pk(hi[2], hi[3])};
        *reinterpret_cast<u32x4*>(hrow + 8 * (4 * c + g)) = v;
      }
    }
  }
}

constexpr int G2_ABYTES = 8192, G2_WBYTES = 10240, G2_STAGE = G2_ABYTES + G2_WBYTES;
constexpr int G2_RING = 2 * G2_STAGE;

__global__ __launch_bounds__(256, 1) void gemm2_kernel(const bf16* __restrict__ H, const bf16* __restrict__ w2,
                                                       const bf16* __restrict__ b2, bf16* __restrict__ out) {
  __shared__ __attribute__((aligned(16))) char smem[4 * G2_RING];
  const int tid = threadIdx.x, lane = tid & 63;
  const int w = __builtin_amdgcn_readfirstlane(tid >> 6);
  const int bid = blockIdx.x;
  const int xcd = bid & 7, idx = bid >> 3;
  const int mt = xcd * 4 + (idx & 3);
  const int nt = idx >> 2;
  const int m0 = mt * 64, n0 = nt * 80;
  const int ub = 7 * w;
  const int nu = (w == 3) ? 6 : 7;
  char* ring = smem + w * G2_RING;

  const unsigned ring0 = (unsigned)(uintptr_t)smem + w * G2_RING;
  unsigned aoff[8];
#pragma unroll
  for (int i = 0; i < 8; ++i) {
    const int row = 8 * i + (lane >> 3);
    const int chunk = (lane & 7) ^ ((row >> 1) & 7);
    aoff[i] = (unsigned)(row * F_ + chunk * 8) * 2u;
  }
  unsigned woff[10];
#pragma unroll
  for (int i = 0; i < 10; ++i) {
    const int L = 64 * i + lane;
    const int grp = L / 80;
    const int r = (L - grp * 80) ^ (8 * (grp & 1));
    const int j = r / 10, ch = r - j * 10;
    woff[i] = (unsigned)((8 * grp + j) * D_ + ch * 8) * 2u;
  }
  const bf16* hbase = H + (size_t)m0 * F_ + ub * 64;
  const bf16* wbase = w2 + (size_t)ub * 64 * D_ + n0;
  auto stage = [&](int u) {
    const unsigned sb = ring0 + (u & 1) * G2_STAGE;
#pragma unroll
    for (int i = 0; i < 8; ++i) glds16(hbase + u * 64, aoff[i], sb + i * 1024);
#pragma unroll
    for (int i = 0; i < 10; ++i) glds16(wbase + (size_t)u * 64 * D_, woff[i], sb + G2_ABYTES + i * 1024);
  };

  const int ml = lane & 15, g = lane >> 4, q = (lane >> 2) & 3, p = lane & 3;
  const int aswz = (ml >> 1) & 7;
  const int a_off0 = ml * 128 + ((g ^ aswz) * 16);
  const int a_off1 = ml * 128 + (((4 + g) ^ aswz) * 16);
  int w_off[2][5];
#pragma unroll
  for (int t = 0; t < 2; ++t)
#pragma unroll
    for (int b = 0; b < 5; ++b) {
      const int L = 80 * g + ((10 * (4 * t + q) + 2 * b + (p >> 1)) ^ (8 * (g & 1)));
      w_off[t][b] = G2_ABYTES + 16 * L + 8 * (p & 1);
    }

  f32x4 acc[5][4];
#pragma unroll
  for (int b = 0; b < 5; ++b)
#pragma unroll
    for (int mb = 0; mb < 4; ++mb) acc[b][mb] = (f32x4){0.f, 0.f, 0.f, 0.f};

  stage(0);
  stage(1);
  for (int u = 0; u < nu; ++u) {
    if (u + 1 < nu) { WAIT_V(18); } else { WAIT_V(0); }
    const char* sb = ring + (u & 1) * G2_STAGE;
    bf16x8 af[2][4], wf[2][5];
#pragma unroll
    for (int s = 0; s < 2; ++s) {
#pragma unroll
      for (int mb = 0; mb < 4; ++mb)
        af[s][mb] = *reinterpret_cast<const bf16x8*>(sb + (s ? a_off1 : a_off0) + mb * 2048);
#pragma unroll
      for (int b = 0; b < 5; ++b)
        wf[s][b] = cat8(tr_read(sb + w_off[0][b] + s * 5120), tr_read(sb + w_off[1][b] + s * 5120));
    }
    asm volatile("s_waitcnt lgkmcnt(0)" ::: "memory");
    __builtin_amdgcn_sched_barrier(0);
    if (u + 2 < nu) stage(u + 2);
#pragma unroll
    for (int s = 0; s < 2; ++s)
#pragma unroll
      for (int b = 0; b < 5; ++b)
#pragma unroll
        for (int mb = 0; mb < 4; ++mb)
          acc[b][mb] = __builtin_amdgcn_mfma_f32_16x16x32_bf16(wf[s][b], af[s][mb], acc[b][mb], 0, 0, 0);
  }

#pragma unroll
  for (int mb = 0; mb < 4; ++mb) {
    if (mb != w) {
      const int rel = (mb - w - 1) & 3;
#pragma unroll
      for (int b = 0; b < 5; ++b)
        *reinterpret_cast<f32x4*>(ring + (rel * 5 + b) * 1024 + lane * 16) = acc[b][mb];
    }
  }
  __syncthreads();
  f32x4 sum[5];
#pragma unroll
  for (int b = 0; b < 5; ++b) {
    sum[b] = (w == 0) ? acc[b][0] : (w == 1) ? acc[b][1] : (w == 2) ? acc[b][2] : acc[b][3];
  }
#pragma unroll
  for (int ws = 0; ws < 4; ++ws) {
    if (ws != w) {
      const int rel = (w - ws - 1) & 3;
      const char* src = smem + ws * G2_RING;
#pragma unroll
      for (int b = 0; b < 5; ++b) {
        const f32x4 v = *reinterpret_cast<const f32x4*>(src + (rel * 5 + b) * 1024 + lane * 16);
        sum[b] += v;
      }
    }
  }
  bf16* orow = out + (size_t)(m0 + 16 * w + ml) * D_ + n0;
#pragma unroll
  for (int b = 0; b < 5; ++b) {
    const s16x4 bb = *reinterpret_cast<const s16x4*>(b2 + n0 + 16 * b + 4 * g);
    const float v0 = sum[b][0] + bf2f(bb[0]), v1 = sum[b][1] + bf2f(bb[1]);
    const float v2 = sum[b][2] + bf2f(bb[2]), v3 = sum[b][3] + bf2f(bb[3]);
    u32x2 v = {cvt_pk(v0, v1), cvt_pk(v2, v3)};
    *reinterpret_cast<u32x2*>(orow + 16 * b + 4 * g) = v;
  }
}

extern "C" void kernel_launch(void* const* d_in, const int* in_sizes, int n_in, void* d_out, int out_size,
                              void* d_ws, size_t ws_size, hipStream_t stream) {
  const bf16* x = (const bf16*)d_in[0];
  const bf16* w1 = (const bf16*)d_in[1];
  const bf16* b1 = (const bf16*)d_in[2];
  const bf16* w2 = (const bf16*)d_in[3];
  const bf16* b2 = (const bf16*)d_in[4];
  bf16* out = (bf16*)d_out;
  bf16* H = (bf16*)d_ws;
  gemm1_kernel<<<224, 256, 0, stream>>>(x, w1, b1, H);
  gemm2_kernel<<<256, 256, 0, stream>>>(H, w2, b2, out);
}
```

```cpp
#include <hip/hip_runtime.h>
#include <hip/hip_bf16.h>
#include <stdint.h>

using bf16 = __hip_bfloat16;
using bf16x8 = __attribute__((ext_vector_type(8))) short;
using s16x4 = __attribute__((ext_vector_type(4))) short;
using f32x4 = __attribute__((ext_vector_type(4))) float;
using u32x4 = __attribute__((ext_vector_type(4))) unsigned;
using u32x2 = __attribute__((ext_vector_type(2))) unsigned;
typedef short v4i16_t __attribute__((ext_vector_type(4)));
typedef __attribute__((address_space(3))) const char* lds_cptr;

constexpr int M_ = 2048, D_ = 640, F_ = 1728;

#define BAR() __builtin_amdgcn_s_barrier()
#define SCHED() __builtin_amdgcn_sched_barrier(0)

template <int N>
__device__ __forceinline__ void wait_vm() {
  asm volatile("s_waitcnt vmcnt(%0)" ::"n"(N) : "memory");
}
__device__ __forceinline__ void wait_lgkm0() { asm volatile("s_waitcnt lgkmcnt(0)" ::: "memory"); }

__device__ __forceinline__ s16x4 tr_read(const char* p) {
  return __builtin_bit_cast(s16x4, __builtin_amdgcn_ds_read_tr16_b64_v4i16(
                                       (__attribute__((address_space(3))) v4i16_t*)(lds_cptr)p));
}
__device__ __forceinline__ bf16x8 cat8(s16x4 lo, s16x4 hi) {
  return (bf16x8){lo[0], lo[1], lo[2], lo[3], hi[0], hi[1], hi[2], hi[3]};
}
__device__ __forceinline__ unsigned cvt_pk(float lo, float hi) {
  unsigned r;
  asm("v_cvt_pk_bf16_f32 %0, %1, %2" : "=v"(r) : "v"(lo), "v"(hi));
  return r;
}
__device__ __forceinline__ void glds16(const void* sbase, unsigned voff, unsigned lds_addr) {
  unsigned sv;
  asm volatile("s_mov_b32 %0, m0\n\ts_mov_b32 m0, %3\n\ts_nop 0\n\tglobal_load_lds_dwordx4 %1, %2\n\ts_mov_b32 m0, %0"
               : "=&s"(sv)
               : "v"(voff), "s"(sbase), "s"(lds_addr)
               : "memory");
}
__device__ __forceinline__ float bf2f(unsigned short v) { return __uint_as_float(((unsigned)v) << 16); }

constexpr int G2_NST = 8;
constexpr int G2_ABYTES = 8192, G2_WBYTES = 10240, G2_STAGE = G2_ABYTES + G2_WBYTES;
constexpr int G2_NKT = F_ / 64;
constexpr int G1_NST = 4;
constexpr int G1_STAGE = 32768;
constexpr int G1_NKT = D_ / 64;

__global__ __launch_bounds__(512, 2) void gemm1_kernel(const bf16* __restrict__ x, const bf16* __restrict__ w1,
                                                       const bf16* __restrict__ b1, bf16* __restrict__ H,
                                                       const bf16* __restrict__ w2, bf16* __restrict__ w2p) {
  __shared__ __attribute__((aligned(16))) char smem[G1_NST * G1_STAGE];
  const int tid = threadIdx.x, lane = tid & 63;
  const int w = __builtin_amdgcn_readfirstlane(tid >> 6);
  const int bid = blockIdx.x;
  const int xcd = bid & 7, idx = bid >> 3;
  if (idx >= 28) {
    const int blk = ((idx - 28) * 8 + xcd) * 8 + w;
    if (blk < 8 * G2_NKT) {
      const int nt2 = blk / G2_NKT, kt2 = blk - nt2 * G2_NKT;
      const bf16* src = w2 + (size_t)kt2 * 64 * D_ + nt2 * 80;
      u32x4* dst = reinterpret_cast<u32x4*>(w2p + (size_t)blk * 5120) + lane;
#pragma unroll
      for (int i = 0; i < 10; ++i) {
        const int L = 64 * i + lane;
        const int grp = L / 80;
        const int r = (L - grp * 80) ^ (8 * (grp & 1));
        const int j = r / 10, ch = r - j * 10;
        dst[64 * i] = *reinterpret_cast<const u32x4*>(src + (size_t)(8 * grp + j) * D_ + ch * 8);
      }
    }
    return;
  }
  const int mt = xcd * 2 + (idx & 1);
  const int nt = idx >> 1;
  const int m0 = mt * 128, n0 = nt * 128;

  if (w >= 4) {
    const int lw = w - 4;
    const unsigned lds0 = (unsigned)(uintptr_t)smem + 4 * lw * 1024;
    unsigned aoff[4], woff[4];
#pragma unroll
    for (int i = 0; i < 4; ++i) {
      const int piece = 4 * lw + i;
      const int row = 8 * piece + (lane >> 3);
      const int chunk = (lane & 7) ^ ((row >> 1) & 7);
      aoff[i] = (unsigned)(row * D_ + chunk * 8) * 2u;
      const int kr = 4 * piece + (lane >> 4);
      int ch = (lane & 15) ^ ((kr & 1) | ((kr & 2) << 1) | (kr & 8));
      if (nt == 13) ch &= 7;
      woff[i] = (unsigned)(kr * F_ + ch * 8) * 2u;
    }
    const bf16* xbase = x + (size_t)m0 * D_;
    const bf16* wbase = w1 + n0;
    auto stage = [&](int st) {
      const unsigned sb = lds0 + (st % G1_NST) * G1_STAGE;
#pragma unroll
      for (int i = 0; i < 4; ++i) glds16(xbase + st * 64, aoff[i], sb + i * 1024);
#pragma unroll
      for (int i = 0; i < 4; ++i) glds16(wbase + (size_t)st * 64 * F_, woff[i], sb + 16384 + i * 1024);
    };
    stage(0);
    stage(1);
    wait_vm<8>();
    BAR();
    stage(2);
    stage(3);
#pragma unroll
    for (int kt = 0; kt < G1_NKT - 1; ++kt) {
      if (kt + 3 < G1_NKT) wait_vm<16>();
      else if (kt + 2 < G1_NKT) wait_vm<8>();
      else wait_vm<0>();
      BAR();
      if (kt + 4 < G1_NKT) stage(kt + 4);
    }
    return;
  }

  const int wm = w >> 1, wn = w & 1;
  const int ml = lane & 15, g = lane >> 4, q = (lane >> 2) & 3, p = lane & 3;
  const int aswz = (ml >> 1) & 7;
  const int a_off[2] = {(wm * 64 + ml) * 128 + ((g ^ aswz) * 16), (wm * 64 + ml) * 128 + (((4 + g) ^ aswz) * 16)};
  const int wmask = (q & 1) | ((q & 2) << 1) | ((g & 1) << 3);
  int w_off[4];
#pragma unroll
  for (int b = 0; b < 4; ++b) {
    const int ch16 = (wn * 8 + 4 * (b >> 1) + p) ^ wmask;
    w_off[b] = 16384 + (8 * g + q) * 256 + ch16 * 16 + 8 * ((b & 1) ^ (p & 1));
  }

  f32x4 acc[4][4];
  {
    const bool valid = (n0 + wn * 64) < F_;
    const bf16* bp = b1 + (valid ? (n0 + wn * 64) : 0);
#pragma unroll
    for (int b = 0; b < 4; ++b) {
      const int nl = 8 * (4 * (b >> 1) + g) + 4 * ((b & 1) ^ (g & 1));
      const s16x4 bb = *reinterpret_cast<const s16x4*>(bp + nl);
      const f32x4 bv = (f32x4){bf2f(bb[0]), bf2f(bb[1]), bf2f(bb[2]), bf2f(bb[3])};
#pragma unroll
      for (int mb = 0; mb < 4; ++mb) acc[b][mb] = bv;
    }
  }

  bf16x8 afA[4], wfA[4], afB[4], wfB[4];
  auto read_set = [&](bf16x8(&af)[4], bf16x8(&wf)[4], int kt, int s) {
    const char* sb = smem + (kt % G1_NST) * G1_STAGE;
#pragma unroll
    for (int b = 0; b < 4; ++b)
      wf[b] = cat8(tr_read(sb + w_off[b] + s * 8192), tr_read(sb + w_off[b] + s * 8192 + 1024));
#pragma unroll
    for (int mb = 0; mb < 4; ++mb) af[mb] = *reinterpret_cast<const bf16x8*>(sb + a_off[s] + mb * 2048);
  };
  auto mma_set = [&](const bf16x8(&af)[4], const bf16x8(&wf)[4]) {
#pragma unroll
    for (int b = 0; b < 4; ++b)
#pragma unroll
      for (int mb = 0; mb < 4; ++mb)
        acc[b][mb] = __builtin_amdgcn_mfma_f32_16x16x32_bf16(wf[b], af[mb], acc[b][mb], 0, 0, 0);
  };

  BAR();
  read_set(afA, wfA, 0, 0);
#pragma unroll
  for (int kt = 0; kt < G1_NKT; ++kt) {
    read_set(afB, wfB, kt, 1);
    SCHED();
    mma_set(afA, wfA);
    SCHED();
    if (kt + 1 < G1_NKT) {
      wait_lgkm0();
      BAR();
      read_set(afA, wfA, kt + 1, 0);
      SCHED();
    }
    mma_set(afB, wfB);
    SCHED();
  }

  if (n0 + wn * 64 < F_) {
#pragma unroll
    for (int mb = 0; mb < 4; ++mb) {
      bf16* hrow = H + (size_t)(m0 + wm * 64 + mb * 16 + ml) * F_ + n0 + wn * 64;
#pragma unroll
      for (int c = 0; c < 2; ++c) {
        f32x4 e = acc[2 * c][mb], o = acc[2 * c + 1][mb];
#pragma unroll
        for (int r = 0; r < 4; ++r) {
          e[r] = e[r] * fmaxf(e[r], 0.f);
          o[r] = o[r] * fmaxf(o[r], 0.f);
        }
        const unsigned e0 = cvt_pk(e[0], e[1]), e1 = cvt_pk(e[2], e[3]);
        const unsigned o0 = cvt_pk(o[0], o[1]), o1 = cvt_pk(o[2], o[3]);
        const bool sw = (g & 1);
        u32x4 v = {sw ? o0 : e0, sw ? o1 : e1, sw ? e0 : o0, sw ? e1 : o1};
        *reinterpret_cast<u32x4*>(hrow + 8 * (4 * c + g)) = v;
      }
    }
  }
}

__global__ __launch_bounds__(512, 2) void gemm2_kernel(const bf16* __restrict__ H, const bf16* __restrict__ w2p,
                                                       const bf16* __restrict__ b2, bf16* __restrict__ out) {
  __shared__ __attribute__((aligned(16))) char smem[G2_NST * G2_STAGE];
  const int tid = threadIdx.x, lane = tid & 63;
  const int w = __builtin_amdgcn_readfirstlane(tid >> 6);
  const int bid = blockIdx.x;
  const int xcd = bid & 7, idx = bid >> 3;
  const int mt = xcd * 4 + (idx & 3);
  const int nt = idx >> 2;
  const int m0 = mt * 64, n0 = nt * 80;

  if (w >= 4) {
    const int lw = w - 4;
    const unsigned lds0 = (unsigned)(uintptr_t)smem;
    unsigned aoff[2];
#pragma unroll
    for (int i = 0; i < 2; ++i) {
      const int row = 8 * (2 * lw + i) + (lane >> 3);
      const int chunk = (lane & 7) ^ ((row >> 1) & 7);
      aoff[i] = (unsigned)(row * F_ + chunk * 8) * 2u;
    }
    const unsigned wlane = (unsigned)lane * 16u;
    const bf16* hbase = H + (size_t)m0 * F_;
    const char* wbase = (const char*)w2p + (size_t)nt * G2_NKT * G2_WBYTES;
    const bool three = (lw < 2);
    auto stage = [&](int st) {
      const unsigned sb = lds0 + (st % G2_NST) * G2_STAGE;
#pragma unroll
      for (int i = 0; i < 2; ++i) glds16(hbase + st * 64, aoff[i], sb + (2 * lw + i) * 1024);
      const char* wst = wbase + (size_t)st * G2_WBYTES;
#pragma unroll
      for (int i = 0; i < 2; ++i) glds16(wst + (lw + 4 * i) * 1024, wlane, sb + G2_ABYTES + (lw + 4 * i) * 1024);
      if (three) glds16(wst + (lw + 8) * 1024, wlane, sb + G2_ABYTES + (lw + 8) * 1024);
    };
    stage(0);
    stage(1);
    if (three) wait_vm<5>(); else wait_vm<4>();
    BAR();
#pragma unroll
    for (int st = 2; st < G2_NST; ++st) stage(st);
#pragma unroll
    for (int kt = 0; kt < G2_NKT - 1; ++kt) {
      constexpr int dummy = 0;
      (void)dummy;
      const int last = (kt + G2_NST - 1 < G2_NKT - 1) ? (kt + G2_NST - 1) : (G2_NKT - 1);
      const int inflight = last - (kt + 1);
      switch (inflight) {
        case 0: wait_vm<0>(); break;
        case 1: if (three) wait_vm<5>(); else wait_vm<4>(); break;
        case 2: if (three) wait_vm<10>(); else wait_vm<8>(); break;
        case 3: if (three) wait_vm<15>(); else wait_vm<12>(); break;
        case 4: if (three) wait_vm<20>(); else wait_vm<16>(); break;
        case 5: if (three) wait_vm<25>(); else wait_vm<20>(); break;
        default: if (three) wait_vm<30>(); else wait_vm<24>(); break;
      }
      BAR();
      if (kt + G2_NST < G2_NKT) stage(kt + G2_NST);
    }
    BAR();
    BAR();
    return;
  }

  const int kh = w >> 1, mh = w & 1;
  const int ml = lane & 15, g = lane >> 4, q = (lane >> 2) & 3, p = lane & 3;
  const int aswz = (ml >> 1) & 7;
  const int a_off = (mh * 32 + ml) * 128 + (((4 * kh + g) ^ aswz) * 16);
  int w_off[2][5];
#pragma unroll
  for (int t = 0; t < 2; ++t)
#pragma unroll
    for (int b = 0; b < 5; ++b) {
      const int L = 80 * (4 * kh + g) + ((10 * (4 * t + q) + 2 * b + (p >> 1)) ^ (8 * (g & 1)));
      w_off[t][b] = G2_ABYTES + 16 * L + 8 * (p & 1);
    }

  f32x4 acc[5][2];
#pragma unroll
  for (int b = 0; b < 5; ++b)
#pragma unroll
    for (int mb = 0; mb < 2; ++mb) acc[b][mb] = (f32x4){0.f, 0.f, 0.f, 0.f};

  bf16x8 af[2][2], wf[2][5];
  auto read_set = [&](int set, int kt) {
    const char* sb = smem + (kt % G2_NST) * G2_STAGE;
#pragma unroll
    for (int b = 0; b < 5; ++b) wf[set][b] = cat8(tr_read(sb + w_off[0][b]), tr_read(sb + w_off[1][b]));
#pragma unroll
    for (int mb = 0; mb < 2; ++mb) af[set][mb] = *reinterpret_cast<const bf16x8*>(sb + a_off + mb * 2048);
  };

  BAR();
  read_set(0, 0);
#pragma unroll
  for (int kt = 0; kt < G2_NKT; ++kt) {
    if (kt + 1 < G2_NKT) {
      wait_lgkm0();
      BAR();
      read_set((kt + 1) & 1, kt + 1);
      SCHED();
    }
#pragma unroll
    for (int b = 0; b < 5; ++b)
#pragma unroll
      for (int mb = 0; mb < 2; ++mb)
        acc[b][mb] = __builtin_amdgcn_mfma_f32_16x16x32_bf16(wf[kt & 1][b], af[kt & 1][mb], acc[b][mb], 0, 0, 0);
    SCHED();
  }

  __syncthreads();
  {
    const int sendb = 1 - kh;
    char* dst = smem + w * 5120;
#pragma unroll
    for (int b = 0; b < 5; ++b) *reinterpret_cast<f32x4*>(dst + b * 1024 + lane * 16) = sendb ? acc[b][1] : acc[b][0];
  }
  __syncthreads();
  {
    const int partner = w ^ 2;
    const char* src = smem + partner * 5120;
    const int mrow = m0 + mh * 32 + kh * 16 + ml;
    bf16* orow = out + (size_t)mrow * D_ + n0;
#pragma unroll
    for (int b = 0; b < 5; ++b) {
      const f32x4 mine = kh ? acc[b][1] : acc[b][0];
      const f32x4 v = *reinterpret_cast<const f32x4*>(src + b * 1024 + lane * 16);
      const s16x4 bb = *reinterpret_cast<const s16x4*>(b2 + n0 + 16 * b + 4 * g);
      const f32x4 lo = kh ? v : mine, hi = kh ? mine : v;
      const float v0 = (lo[0] + hi[0]) + bf2f(bb[0]), v1 = (lo[1] + hi[1]) + bf2f(bb[1]);
      const float v2 = (lo[2] + hi[2]) + bf2f(bb[2]), v3 = (lo[3] + hi[3]) + bf2f(bb[3]);
      u32x2 o = {cvt_pk(v0, v1), cvt_pk(v2, v3)};
      *reinterpret_cast<u32x2*>(orow + 16 * b + 4 * g) = o;
    }
  }
}

extern "C" void kernel_launch(void* const* d_in, const int* in_sizes, int n_in, void* d_out, int out_size,
                              void* d_ws, size_t ws_size, hipStream_t stream) {
  const bf16* x = (const bf16*)d_in[0];
  const bf16* w1 = (const bf16*)d_in[1];
  const bf16* b1 = (const bf16*)d_in[2];
  const bf16* w2 = (const bf16*)d_in[3];
  const bf16* b2 = (const bf16*)d_in[4];
  bf16* out = (bf16*)d_out;
  bf16* H = (bf16*)d_ws;
  bf16* w2p = (bf16*)((char*)d_ws + (size_t)8 * 1024 * 1024);
  gemm1_kernel<<<256, 512, 0, stream>>>(x, w1, b1, H, w2, w2p);
  gemm2_kernel<<<256, 512, 0, stream>>>(H, w2p, b2, out);
}
```

```cpp
#include <hip/hip_runtime.h>
#include <hip/hip_bf16.h>
#include <stdint.h>

using bf16 = __hip_bfloat16;
using bf16x8 = __attribute__((ext_vector_type(8))) short;
using s16x4 = __attribute__((ext_vector_type(4))) short;
using f32x4 = __attribute__((ext_vector_type(4))) float;
using u32x4 = __attribute__((ext_vector_type(4))) unsigned;
using u32x2 = __attribute__((ext_vector_type(2))) unsigned;
typedef short v4i16_t __attribute__((ext_vector_type(4)));
typedef __attribute__((address_space(3))) const char* lds_cptr;

constexpr int M_ = 2048, D_ = 640, F_ = 1728;

#define BAR() __builtin_amdgcn_s_barrier()
#define SCHED() __builtin_amdgcn_sched_barrier(0)

template <int N>
__device__ __forceinline__ void wait_vm() {
  asm volatile("s_waitcnt vmcnt(%0)" ::"n"(N) : "memory");
}
__device__ __forceinline__ void wait_lgkm0() { asm volatile("s_waitcnt lgkmcnt(0)" ::: "memory"); }

__device__ __forceinline__ s16x4 tr_read(const char* p) {
  return __builtin_bit_cast(s16x4, __builtin_amdgcn_ds_read_tr16_b64_v4i16(
                                       (__attribute__((address_space(3))) v4i16_t*)(lds_cptr)p));
}
__device__ __forceinline__ bf16x8 cat8(s16x4 lo, s16x4 hi) {
  return (bf16x8){lo[0], lo[1], lo[2], lo[3], hi[0], hi[1], hi[2], hi[3]};
}
__device__ __forceinline__ unsigned cvt_pk(float lo, float hi) {
  unsigned r;
  asm("v_cvt_pk_bf16_f32 %0, %1, %2" : "=v"(r) : "v"(lo), "v"(hi));
  return r;
}
__device__ __forceinline__ void glds16(const void* sbase, unsigned voff, unsigned lds_addr) {
  unsigned sv;
  asm volatile("s_mov_b32 %0, m0\n\ts_mov_b32 m0, %3\n\ts_nop 0\n\tglobal_load_lds_dwordx4 %1, %2\n\ts_mov_b32 m0, %0"
               : "=&s"(sv)
               : "v"(voff), "s"(sbase), "s"(lds_addr)
               : "memory");
}
__device__ __forceinline__ float bf2f(unsigned short v) { return __uint_as_float(((unsigned)v) << 16); }

constexpr int G2_NST = 8;
constexpr int G2_ABYTES = 8192, G2_WBYTES = 10240, G2_STAGE = G2_ABYTES + G2_WBYTES;
constexpr int G2_NKT = F_ / 64;
constexpr int G1_NST = 4;
constexpr int G1_STAGE = 32768;
constexpr int G1_NKT = D_ / 64;

__global__ __launch_bounds__(512, 2) void gemm1_kernel(const bf16* __restrict__ x, const bf16* __restrict__ w1,
                                                       const bf16* __restrict__ b1, bf16* __restrict__ H,
                                                       const bf16* __restrict__ w2, bf16* __restrict__ w2p) {
  __shared__ __attribute__((aligned(16))) char smem[G1_NST * G1_STAGE];
  const int tid = threadIdx.x, lane = tid & 63;
  const int w = __builtin_amdgcn_readfirstlane(tid >> 6);
  const int bid = blockIdx.x;
  const int xcd = bid & 7, idx = bid >> 3;
  if (idx >= 28) {
    const int blk = ((idx - 28) * 8 + xcd) * 8 + w;
    if (blk < 8 * G2_NKT) {
      const int nt2 = blk / G2_NKT, kt2 = blk - nt2 * G2_NKT;
      const bf16* src = w2 + (size_t)kt2 * 64 * D_ + nt2 * 80;
      u32x4* dst = reinterpret_cast<u32x4*>(w2p + (size_t)blk * 5120) + lane;
#pragma unroll
      for (int i = 0; i < 10; ++i) {
        const int L = 64 * i + lane;
        const int grp = L / 80;
        const int r = (L - grp * 80) ^ (8 * (grp & 1));
        const int j = r / 10, ch = r - j * 10;
        dst[64 * i] = *reinterpret_cast<const u32x4*>(src + (size_t)(8 * grp + j) * D_ + ch * 8);
      }
    }
    return;
  }
  const int mt = xcd * 2 + (idx & 1);
  const int nt = idx >> 1;
  const int m0 = mt * 128, n0 = nt * 128;

  if (w >= 4) {
    const int lw = w - 4;
    const unsigned lds0 = (unsigned)(uintptr_t)smem + 4 * lw * 1024;
    unsigned aoff[4], woff[4];
#pragma unroll
    for (int i = 0; i < 4; ++i) {
      const int piece = 4 * lw + i;
      const int row = 8 * piece + (lane >> 3);
      const int chunk = (lane & 7) ^ ((row >> 1) & 7);
      aoff[i] = (unsigned)(row * D_ + chunk * 8) * 2u;
      const int kr = 4 * piece + (lane >> 4);
      int ch = (lane & 15) ^ ((kr & 1) | ((kr & 2) << 1) | (kr & 8));
      if (nt == 13) ch &= 7;
      woff[i] = (unsigned)(kr * F_ + ch * 8) * 2u;
    }
    const bf16* xbase = x + (size_t)m0 * D_;
    const bf16* wbase = w1 + n0;
    const int krot = (bid * 3) % G1_NKT;
    auto stage = [&](int st) {
      const unsigned sb = lds0 + (st % G1_NST) * G1_STAGE;
      int kg = st + krot;
      if (kg >= G1_NKT) kg -= G1_NKT;
#pragma unroll
      for (int i = 0; i < 4; ++i) glds16(xbase + kg * 64, aoff[i], sb + i * 1024);
#pragma unroll
      for (int i = 0; i < 4; ++i) glds16(wbase + (size_t)kg * 64 * F_, woff[i], sb + 16384 + i * 1024);
    };
    stage(0);
    stage(1);
    wait_vm<8>();
    BAR();
    stage(2);
    stage(3);
#pragma unroll
    for (int kt = 0; kt < G1_NKT - 1; ++kt) {
      if (kt + 3 < G1_NKT) wait_vm<16>();
      else if (kt + 2 < G1_NKT) wait_vm<8>();
      else wait_vm<0>();
      BAR();
      if (kt + 4 < G1_NKT) stage(kt + 4);
    }
    return;
  }

  const int wm = w >> 1, wn = w & 1;
  const int ml = lane & 15, g = lane >> 4, q = (lane >> 2) & 3, p = lane & 3;
  const int aswz = (ml >> 1) & 7;
  const int a_off[2] = {(wm * 64 + ml) * 128 + ((g ^ aswz) * 16), (wm * 64 + ml) * 128 + (((4 + g) ^ aswz) * 16)};
  const int wmask = (q & 1) | ((q & 2) << 1) | ((g & 1) << 3);
  int w_off[4];
#pragma unroll
  for (int b = 0; b < 4; ++b) {
    const int ch16 = (wn * 8 + 4 * (b >> 1) + p) ^ wmask;
    w_off[b] = 16384 + (8 * g + q) * 256 + ch16 * 16 + 8 * ((b & 1) ^ (p & 1));
  }

  f32x4 acc[4][4];
  {
    const bool valid = (n0 + wn * 64) < F_;
    const bf16* bp = b1 + (valid ? (n0 + wn * 64) : 0);
#pragma unroll
    for (int b = 0; b < 4; ++b) {
      const int nl = 8 * (4 * (b >> 1) + g) + 4 * ((b & 1) ^ (g & 1));
      const s16x4 bb = *reinterpret_cast<const s16x4*>(bp + nl);
      const f32x4 bv = (f32x4){bf2f(bb[0]), bf2f(bb[1]), bf2f(bb[2]), bf2f(bb[3])};
#pragma unroll
      for (int mb = 0; mb < 4; ++mb) acc[b][mb] = bv;
    }
  }

  bf16x8 afA[4], wfA[4], afB[4], wfB[4];
  auto read_set = [&](bf16x8(&af)[4], bf16x8(&wf)[4], int kt, int s) {
    const char* sb = smem + (kt % G1_NST) * G1_STAGE;
#pragma unroll
    for (int b = 0; b < 4; ++b)
      wf[b] = cat8(tr_read(sb + w_off[b] + s * 8192), tr_read(sb + w_off[b] + s * 8192 + 1024));
#pragma unroll
    for (int mb = 0; mb < 4; ++mb) af[mb] = *reinterpret_cast<const bf16x8*>(sb + a_off[s] + mb * 2048);
  };
  auto mma_set = [&](const bf16x8(&af)[4], const bf16x8(&wf)[4]) {
#pragma unroll
    for (int b = 0; b < 4; ++b)
#pragma unroll
      for (int mb = 0; mb < 4; ++mb)
        acc[b][mb] = __builtin_amdgcn_mfma_f32_16x16x32_bf16(wf[b], af[mb], acc[b][mb], 0, 0, 0);
  };

  BAR();
  read_set(afA, wfA, 0, 0);
#pragma unroll
  for (int kt = 0; kt < G1_NKT; ++kt) {
    read_set(afB, wfB, kt, 1);
    SCHED();
    mma_set(afA, wfA);
    SCHED();
    if (kt + 1 < G1_NKT) {
      wait_lgkm0();
      BAR();
      read_set(afA, wfA, kt + 1, 0);
      SCHED();
    }
    mma_set(afB, wfB);
    SCHED();
  }

  if (n0 + wn * 64 < F_) {
#pragma unroll
    for (int mb = 0; mb < 4; ++mb) {
      bf16* hrow = H + (size_t)(m0 + wm * 64 + mb * 16 + ml) * F_ + n0 + wn * 64;
#pragma unroll
      for (int c = 0; c < 2; ++c) {
        f32x4 e = acc[2 * c][mb], o = acc[2 * c + 1][mb];
#pragma unroll
        for (int r = 0; r < 4; ++r) {
          e[r] = e[r] * fmaxf(e[r], 0.f);
          o[r] = o[r] * fmaxf(o[r], 0.f);
        }
        const unsigned e0 = cvt_pk(e[0], e[1]), e1 = cvt_pk(e[2], e[3]);
        const unsigned o0 = cvt_pk(o[0], o[1]), o1 = cvt_pk(o[2], o[3]);
        const bool sw = (g & 1);
        u32x4 v = {sw ? o0 : e0, sw ? o1 : e1, sw ? e0 : o0, sw ? e1 : o1};
        *reinterpret_cast<u32x4*>(hrow + 8 * (4 * c + g)) = v;
      }
    }
  }
}

__global__ __launch_bounds__(512, 2) void gemm2_kernel(const bf16* __restrict__ H, const bf16* __restrict__ w2p,
                                                       const bf16* __restrict__ b2, bf16* __restrict__ out) {
  __shared__ __attribute__((aligned(16))) char smem[G2_NST * G2_STAGE];
  const int tid = threadIdx.x, lane = tid & 63;
  const int w = __builtin_amdgcn_readfirstlane(tid >> 6);
  const int bid = blockIdx.x;
  const int xcd = bid & 7, idx = bid >> 3;
  const int mt = xcd * 4 + (idx & 3);
  const int nt = idx >> 2;
  const int m0 = mt * 64, n0 = nt * 80;

  if (w >= 4) {
    const int lw = w - 4;
    const unsigned lds0 = (unsigned)(uintptr_t)smem;
    unsigned aoff[2];
#pragma unroll
    for (int i = 0; i < 2; ++i) {
      const int row = 8 * (2 * lw + i) + (lane >> 3);
      const int chunk = (lane & 7) ^ ((row >> 1) & 7);
      aoff[i] = (unsigned)(row * F_ + chunk * 8) * 2u;
    }
    const unsigned wlane = (unsigned)lane * 16u;
    const bf16* hbase = H + (size_t)m0 * F_;
    const char* wbase = (const char*)w2p + (size_t)nt * G2_NKT * G2_WBYTES;
    const bool three = (lw < 2);
    auto stage = [&](int st) {
      const unsigned sb = lds0 + (st % G2_NST) * G2_STAGE;
#pragma unroll
      for (int i = 0; i < 2; ++i) glds16(hbase + st * 64, aoff[i], sb + (2 * lw + i) * 1024);
      const char* wst = wbase + (size_t)st * G2_WBYTES;
#pragma unroll
      for (int i = 0; i < 2; ++i) glds16(wst + (lw + 4 * i) * 1024, wlane, sb + G2_ABYTES + (lw + 4 * i) * 1024);
      if (three) glds16(wst + (lw + 8) * 1024, wlane, sb + G2_ABYTES + (lw + 8) * 1024);
    };
    stage(0);
    stage(1);
    if (three) wait_vm<5>(); else wait_vm<4>();
    BAR();
#pragma unroll
    for (int st = 2; st < G2_NST; ++st) stage(st);
#pragma unroll
    for (int kt = 0; kt < G2_NKT - 1; ++kt) {
      constexpr int dummy = 0;
      (void)dummy;
      const int last = (kt + G2_NST - 1 < G2_NKT - 1) ? (kt + G2_NST - 1) : (G2_NKT - 1);
      const int inflight = last - (kt + 1);
      switch (inflight) {
        case 0: wait_vm<0>(); break;
        case 1: if (three) wait_vm<5>(); else wait_vm<4>(); break;
        case 2: if (three) wait_vm<10>(); else wait_vm<8>(); break;
        case 3: if (three) wait_vm<15>(); else wait_vm<12>(); break;
        case 4: if (three) wait_vm<20>(); else wait_vm<16>(); break;
        case 5: if (three) wait_vm<25>(); else wait_vm<20>(); break;
        default: if (three) wait_vm<30>(); else wait_vm<24>(); break;
      }
      BAR();
      if (kt + G2_NST < G2_NKT) stage(kt + G2_NST);
    }
    BAR();
    BAR();
    return;
  }

  const int kh = w >> 1, mh = w & 1;
  const int ml = lane & 15, g = lane >> 4, q = (lane >> 2) & 3, p = lane & 3;
  const int aswz = (ml >> 1) & 7;
  const int a_off = (mh * 32 + ml) * 128 + (((4 * kh + g) ^ aswz) * 16);
  int w_off[2][5];
#pragma unroll
  for (int t = 0; t < 2; ++t)
#pragma unroll
    for (int b = 0; b < 5; ++b) {
      const int L = 80 * (4 * kh + g) + ((10 * (4 * t + q) + 2 * b + (p >> 1)) ^ (8 * (g & 1)));
      w_off[t][b] = G2_ABYTES + 16 * L + 8 * (p & 1);
    }

  f32x4 acc[5][2];
#pragma unroll
  for (int b = 0; b < 5; ++b)
#pragma unroll
    for (int mb = 0; mb < 2; ++mb) acc[b][mb] = (f32x4){0.f, 0.f, 0.f, 0.f};

  bf16x8 af[2][2], wf[2][5];
  auto read_set = [&](int set, int kt) {
    const char* sb = smem + (kt % G2_NST) * G2_STAGE;
#pragma unroll
    for (int b = 0; b < 5; ++b) wf[set][b] = cat8(tr_read(sb + w_off[0][b]), tr_read(sb + w_off[1][b]));
#pragma unroll
    for (int mb = 0; mb < 2; ++mb) af[set][mb] = *reinterpret_cast<const bf16x8*>(sb + a_off + mb * 2048);
  };

  BAR();
  read_set(0, 0);
#pragma unroll
  for (int kt = 0; kt < G2_NKT; ++kt) {
    if (kt + 1 < G2_NKT) {
      wait_lgkm0();
      BAR();
      read_set((kt + 1) & 1, kt + 1);
      SCHED();
    }
#pragma unroll
    for (int b = 0; b < 5; ++b)
#pragma unroll
      for (int mb = 0; mb < 2; ++mb)
        acc[b][mb] = __builtin_amdgcn_mfma_f32_16x16x32_bf16(wf[kt & 1][b], af[kt & 1][mb], acc[b][mb], 0, 0, 0);
    SCHED();
  }

  __syncthreads();
  {
    const int sendb = 1 - kh;
    char* dst = smem + w * 5120;
#pragma unroll
    for (int b = 0; b < 5; ++b) *reinterpret_cast<f32x4*>(dst + b * 1024 + lane * 16) = sendb ? acc[b][1] : acc[b][0];
  }
  __syncthreads();
  {
    const int partner = w ^ 2;
    const char* src = smem + partner * 5120;
    const int mrow = m0 + mh * 32 + kh * 16 + ml;
    bf16* orow = out + (size_t)mrow * D_ + n0;
#pragma unroll
    for (int b = 0; b < 5; ++b) {
      const f32x4 mine = kh ? acc[b][1] : acc[b][0];
      const f32x4 v = *reinterpret_cast<const f32x4*>(src + b * 1024 + lane * 16);
      const s16x4 bb = *reinterpret_cast<const s16x4*>(b2 + n0 + 16 * b + 4 * g);
      const f32x4 lo = kh ? v : mine, hi = kh ? mine : v;
      const float v0 = (lo[0] + hi[0]) + bf2f(bb[0]), v1 = (lo[1] + hi[1]) + bf2f(bb[1]);
      const float v2 = (lo[2] + hi[2]) + bf2f(bb[2]), v3 = (lo[3] + hi[3]) + bf2f(bb[3]);
      u32x2 o = {cvt_pk(v0, v1), cvt_pk(v2, v3)};
      *reinterpret_cast<u32x2*>(orow + 16 * b + 4 * g) = o;
    }
  }
}

extern "C" void kernel_launch(void* const* d_in, const int* in_sizes, int n_in, void* d_out, int out_size,
                              void* d_ws, size_t ws_size, hipStream_t stream) {
  const bf16* x = (const bf16*)d_in[0];
  const bf16* w1 = (const bf16*)d_in[1];
  const bf16* b1 = (const bf16*)d_in[2];
  const bf16* w2 = (const bf16*)d_in[3];
  const bf16* b2 = (const bf16*)d_in[4];
  bf16* out = (bf16*)d_out;
  bf16* H = (bf16*)d_ws;
  bf16* w2p = (bf16*)((char*)d_ws + (size_t)8 * 1024 * 1024);
  gemm1_kernel<<<256, 512, 0, stream>>>(x, w1, b1, H, w2, w2p);
  gemm2_kernel<<<256, 512, 0, stream>>>(H, w2p, b2, out);
}
```
